```python
import torch

def _softmax(x, axis=-1):
    return torch.softmax(x, dim=axis)
def _gelu(x):
    return torch.nn.functional.gelu(x, approximate="tanh")
def _silu(x):
    return torch.nn.functional.silu(x)
def _sigmoid(x):
    return torch.sigmoid(x)
def _layernorm(x, axis=-1, eps=1e-5):
    m = x.mean(dim=axis, keepdim=True)
    v = x.var(dim=axis, keepdim=True, unbiased=False)
    return (x - m) / torch.sqrt(v + eps)
def _scatter_add(tgt, idx, src, axis=0):
    return tgt.clone().index_add_(axis, idx, src)
def _segment_sum(src, seg, n):
    out = torch.zeros((n,) + src.shape[1:], dtype=src.dtype)
    return out.index_add_(0, seg, src)
def _pad(x, pw):
    flat = tuple(v for pair in reversed(pw) for v in pair)
    return torch.nn.functional.pad(x, flat)

kernel_name = 'parallel_sum__linear_kernel_hedgehog__dense_activation_relu__prompt'

def setup_inputs():

    _w = (lambda a: (lambda t: t.to(torch.bfloat16) if t.is_floating_point() else t)(torch.as_tensor(a)))
    def _A_setup():
        B, S, D, H = 1, 2048, 576, 32
        Dh = D // H
        return {
            "x": _w((lambda *s: torch.randn(*s, dtype=torch.bfloat16) * 0.05)(B, S, D)),
            "wqkv": _w((lambda *s: torch.randn(*s, dtype=torch.bfloat16) * 0.8)(D, 3 * D)),
            "w_phi": _w((lambda *s: torch.randn(*s, dtype=torch.bfloat16) * 0.8)(H, Dh, Dh) * (Dh ** -0.5)),
            "wo": _w((lambda *s: torch.randn(*s, dtype=torch.bfloat16) * 0.2)(D, D)),
        }
    _A_in = _A_setup()

    def _B_setup():
        B, S, D, Dff = 1, 2048, 576, 4608
        return {
            "x": _w((lambda *s: torch.randn(*s, dtype=torch.bfloat16) * 0.05)(B, S, D)),
            "w1": _w((lambda *s: torch.randn(*s, dtype=torch.bfloat16) * 0.05)(D, Dff)),
            "w2": _w((lambda *s: torch.randn(*s, dtype=torch.bfloat16) * 0.05)(Dff, D)),
        }
    _B_in = _B_setup()

    return {
        "A_x": _A_in["x"],
        "A_wqkv": _A_in["wqkv"],
        "A_w_phi": _A_in["w_phi"],
        "A_wo": _A_in["wo"],
        "B_x": _B_in["x"],
        "B_w1": _B_in["w1"],
        "B_w2": _B_in["w2"],
    }

def reference(A_x, A_wqkv, A_w_phi, A_wo, B_x, B_w1, B_w2):
    def _A_ref(x, wqkv, w_phi, wo):
        B, S, D = x.shape
        H = 32; Dh = D // H
        qkv = (lambda x, *d: x.permute(*d))((x @ wqkv).reshape(B, S, 3, H, Dh), 2, 0, 3, 1, 4)
        q, k, v = qkv[0], qkv[1], qkv[2]
        pq = torch.einsum("bhsd,hdf->bhsf", q, w_phi)
        pk = torch.einsum("bhsd,hdf->bhsf", k, w_phi)
        phi_q = torch.concatenate([_softmax(pq, axis=-1), _softmax(-pq, axis=-1)], axis=-1)
        phi_k = torch.concatenate([_softmax(pk, axis=-1), _softmax(-pk, axis=-1)], axis=-1)
        kv = torch.einsum("bhsf,bhsd->bhfd", phi_k, v)
        z  = torch.einsum("bhsf->bhf", phi_k)
        num = torch.einsum("bhqf,bhfd->bhqd", phi_q, kv)
        den = torch.einsum("bhqf,bhf->bhq", phi_q, z)[..., None] + 1e-6
        o = num / den
        return (lambda x, *d: x.permute(*d))(o, 0, 2, 1, 3).reshape(B, S, D) @ wo

    def _B_ref(x, w1, w2):
        h = torch.einsum("bsd,df->bsf", x, w1)
        zero = torch.zeros_like(h)
        h = torch.maximum(h, zero)
        return torch.einsum("bsf,fd->bsd", h, w2)

    x = A_x
    xn = _layernorm(x, axis=-1)
    return x + _A_ref(xn, A_wqkv, A_w_phi, A_wo) + _B_ref(xn, B_w1, B_w2)
```

```cpp
#include <hip/hip_runtime.h>
#include <stdint.h>
#include <utility>

typedef __attribute__((ext_vector_type(8))) __bf16 bf16x8;
typedef __attribute__((ext_vector_type(2))) __bf16 bf16x2;
typedef __attribute__((ext_vector_type(4))) short s16x4;
typedef __attribute__((ext_vector_type(2))) short s16x2;
typedef __attribute__((ext_vector_type(8))) short s16x8;
typedef __attribute__((ext_vector_type(16))) float f32x16;
typedef __attribute__((ext_vector_type(4))) float f32x4;
typedef __attribute__((ext_vector_type(2))) float f32x2;
typedef __attribute__((ext_vector_type(4))) unsigned u32x4;
typedef __attribute__((ext_vector_type(2))) unsigned u32x2;
typedef __attribute__((address_space(1))) unsigned gu32;
typedef __amdgpu_buffer_rsrc_t rsrc_t;
#define LDSP(T, off) ((__attribute__((address_space(3))) T*)(size_t)(off))
#define DI __device__ __forceinline__
#define PIN __builtin_amdgcn_sched_barrier(0)

constexpr int S_ = 2048, D_ = 576, DFF_ = 4608, NQKV_ = 1728;
constexpr unsigned HREG = 73728, OFR = 147456, KVT = 159744, LDS_TOTAL = 163840;
#define PFL 1088
#define PFJ 1
constexpr size_t WS_PF = 896u << 10;
constexpr int PF_LINE = PFL, PF_J = PFJ;
constexpr size_t WS_KV = 16384, KV_BYTES = 3072;
constexpr size_t WS_PHI = 1536u << 10;
constexpr size_t WS_WT = 1792u << 10;
constexpr size_t WS_XN = 2u << 20;
constexpr size_t WS_WQ = 5u << 20;
constexpr size_t WS_P = 8u << 20;
constexpr size_t P_SPLIT = (size_t)S_ * D_ * 2;
#define JX 2

DI unsigned pk2(float a, float b) { f32x2 v = {a, b}; return __builtin_bit_cast(unsigned, __builtin_convertvector(v, bf16x2)); }
DI float rbf(float a) { return __uint_as_float(pk2(0.f, a)); }
DI float blo(unsigned u) { return __uint_as_float(u << 16); }
DI float bhi(unsigned u) { return __uint_as_float(u & 0xffff0000u); }
DI unsigned relu2(unsigned u) {
  const s16x2 z = {0, 0};
  return __builtin_bit_cast(unsigned, __builtin_elementwise_max(__builtin_bit_cast(s16x2, u), z));
}
DI f32x16 mfma(bf16x8 a, bf16x8 b, f32x16 c) { return __builtin_amdgcn_mfma_f32_32x32x16_bf16(a, b, c, 0, 0, 0); }
DI f32x16 zero16() { f32x16 z; for (int t = 0; t < 16; ++t) z[t] = 0.f; return z; }
DI bf16x8 trfrag(unsigned a0, unsigned a1) {
  s16x4 lo = __builtin_amdgcn_ds_read_tr16_b64_v4i16(LDSP(s16x4, a0));
  s16x4 hi = __builtin_amdgcn_ds_read_tr16_b64_v4i16(LDSP(s16x4, a1));
  return __builtin_bit_cast(bf16x8, __builtin_shufflevector(lo, hi, 0, 1, 2, 3, 4, 5, 6, 7));
}
DI bf16x8 ldsfrag(unsigned a) { return *LDSP(bf16x8, a); }
DI bf16x8 pack8(const f32x16& x, int s) {
  u32x4 p = {pk2(x[8 * s], x[8 * s + 1]), pk2(x[8 * s + 2], x[8 * s + 3]), pk2(x[8 * s + 4], x[8 * s + 5]), pk2(x[8 * s + 6], x[8 * s + 7])};
  return __builtin_bit_cast(bf16x8, p);
}
template <unsigned MC, unsigned SC> DI void dma(unsigned mb, unsigned voff, rsrc_t rs) {
  unsigned t;
  asm volatile("s_add_u32 m0, %1, %2\n\ts_mov_b32 %0, %3\n\tbuffer_load_dwordx4 %4, %5, %0 offen lds"
               : "=&s"(t) : "s"(mb), "i"(MC), "i"(SC), "v"(voff), "s"(rs) : "memory", "scc");
}
template <unsigned LB> DI void touch(const void* p) {
  asm volatile("s_mov_b32 m0, %1\n\ts_nop 0\n\tglobal_load_lds_dword %0, off" ::"v"(p), "i"(LB) : "memory");
}
constexpr unsigned CODE_LINES = 352;
DI const char* main_code() {
  unsigned long long a;
  int d;
  asm volatile("s_getpc_b64 %0\n\ts_mov_b32 %1, _Z6k_mainPKtS0_S0_S0_PcPt@rel32@lo+4" : "=&s"(a), "=&s"(d));
  return (const char*)a + d;
}
DI unsigned fresh_lane() {
  unsigned l;
  asm volatile("v_mbcnt_lo_u32_b32 %0, -1, 0\n\tv_mbcnt_hi_u32_b32 %0, -1, %0" : "=v"(l));
  return l;
}
DI unsigned opaque(unsigned v) { asm volatile("" : "+v"(v)); return v; }
DI unsigned swz_ch(unsigned l) { return (l & 7) ^ (((l >> 4) & 1) << 2); }
DI unsigned tr_col(unsigned l) { return ((l >> 4) & 1) * 32 + (l & 3) * 8; }
template <class T> DI void keep(T& v) { asm volatile("" : "+v"(v)); }
DI void ld_sc1(u32x4& d, unsigned voff, rsrc_t rs, unsigned soff) {
  asm volatile("buffer_load_dwordx4 %0, %1, %2, %3 offen sc1" : "=v"(d) : "v"(voff), "s"(rs), "s"(soff) : "memory");
}
DI void ld3(u32x4 (&d)[3], unsigned voff, rsrc_t rs, unsigned soff) {
  asm volatile("buffer_load_dwordx4 %0, %3, %4, %5 offen\n\tbuffer_load_dwordx4 %1, %3, %4, %5 offen offset:16\n\tbuffer_load_dwordx4 %2, %3, %4, %5 offen offset:32"
               : "=&v"(d[0]), "=&v"(d[1]), "=&v"(d[2]) : "v"(voff), "s"(rs), "s"(soff) : "memory");
}
DI void ld_flag(unsigned& d, unsigned voff, rsrc_t rs, unsigned soff) {
  asm volatile("buffer_load_dword %0, %1, %2, %3 offen sc1" : "=v"(d) : "v"(voff), "s"(rs), "s"(soff) : "memory");
}
DI void ld_x(u32x2& d, unsigned voff, rsrc_t rs, unsigned soff) {
  asm volatile("buffer_load_dwordx2 %0, %1, %2, %3 offen" : "=v"(d) : "v"(voff), "s"(rs), "s"(soff) : "memory");
}
template <int N> DI void wait_vm() { asm volatile("s_waitcnt vmcnt(%0)" ::"i"(N) : "memory"); }
DI void wait_lds() { asm volatile("s_waitcnt lgkmcnt(0)" ::: "memory"); }
DI void bar() { PIN; asm volatile("s_barrier" ::: "memory"); PIN; }
DI float dpp_sum8(float v) {
  v += __uint_as_float(__builtin_amdgcn_update_dpp(0, __float_as_uint(v), 0xB1, 0xf, 0xf, true));
  v += __uint_as_float(__builtin_amdgcn_update_dpp(0, __float_as_uint(v), 0x4E, 0xf, 0xf, true));
  v += __uint_as_float(__builtin_amdgcn_update_dpp(0, __float_as_uint(v), 0x141, 0xf, 0xf, true));
  return v;
}
DI float half_sum(float v) {
  v = dpp_sum8(v);
  v += __uint_as_float(__builtin_amdgcn_update_dpp(0, __float_as_uint(v), 0x140, 0xf, 0xf, true));
  v += __uint_as_float(__builtin_amdgcn_ds_swizzle(__float_as_uint(v), 0x401F));
  return v;
}
template <int O, class F, int... I> DI void sfor_(F&& f, std::integer_sequence<int, I...>) { (f(std::integral_constant<int, O + I>{}), ...); }
template <int A, int B, class F> DI void sfor(F&& f) { if constexpr (B > A) sfor_<A>(f, std::make_integer_sequence<int, B - A>{}); }

constexpr int NG = 104, NB = 52, PD = 4;
constexpr int BB = 13, BD = 20, BS = 29;
#define PLACE 1
#define W2PRE 0
#define TLBW 1
#define NPRO 9
#define PRB 1
#define PRD 0
#define PRS 0
#define SPLACE 0
#define WHOB 2
#define WHOD 0
#define FLAG_LAG 2
#define CHK_LAG 2
#define SUM_LAG 6
#define NLS 4
constexpr int NA = 108;
struct Item { int kind, idx, hi; };
struct Op { int grp, sub, half, acc, bx, first; };
template <int C> struct Sched {
  Item it[NB + 1][18] = {};
  int nit[NB + 1] = {};
  int wait[NB] = {};
  int pe[3] = {};
  int pu[NB] = {}, fo[NB] = {}, ck[NB] = {}, su[NB] = {}, ls[NB] = {};
  int wait2[NB] = {}, tailw = 0;
  bool late = false;
  Op op[512] = {};
  int o0[NB + 1] = {};
  int nop = 0, s2 = 0;
  static constexpr int extra(int b) {
    if (C == 1) return b == 6 ? 2 : b == BD + 3 ? 12 : b == BD + 7 ? 3 : 0;
    return (b == BB + 1 && !W2PRE) || b == BB + 5 ? 1 : b == BD + 7 ? 3 : 0;
  }
  constexpr void addw(int b, int g) {
    if (g >= NG) return;
    const int who = b > BB && b < BD ? WHOB : b > BD && b < NB ? WHOD : 2;
    if (who == 2) { if (!(g == 31 && C == 1)) it[b][nit[b]++] = Item{0, g, C}; }
    else if (who == C) { it[b][nit[b]++] = Item{0, g, 0}; it[b][nit[b]++] = Item{0, g, 1}; }
  }
  constexpr void addx(int b, int a) { it[b][nit[b]++] = Item{1, a, 2 * C}; it[b][nit[b]++] = Item{1, a, 2 * C + 1}; }
  static constexpr int xread(int a) { return a ? (12 * a - 4) / 8 : 0; }
  static constexpr int xfree(int a) { return a ? (12 * a + 1) / 8 + 1 : 1; }
  constexpr Sched() {
    for (int q = 0; q < 13; ++q) {
      const int row = q < NPRO ? NB : 0;
      if (q % 4 == 0) addx(row, q / 4); else addw(row, q - q / 4 - 1);
    }
    for (int b = 1; b < NB; ++b) {
      for (int a = 0; a < 5; ++a) if (xfree(a) == b) addx(b, a + 4);
      addw(b, 2 * b + 7); addw(b, 2 * b + 8);
    }
    for (int g = 0; g < NG; ++g) {
      if (!(g & 1)) o0[g / 2] = nop;
      if (g < 27) { for (int t = 0; t < 4; ++t) op[nop++] = Op{g, t, C, g % 3, 4 * (g / 3) + t, g < 3 && t == 0}; }
      else if (g < 32) { if (C == 1) for (int t = 0; t < (g == 31 ? 4 : 8); ++t) op[nop++] = Op{g, t, 0, 3, 8 * (g - 27) + t, g == 27 && t == 0}; }
      else if (g < 59) {
        const int blk = (g - 23) / 9;
        if (C == (blk == 1)) for (int t = 0; t < 8; ++t) {
          const int q = 4 * ((g - 23) % 9) + t / 2;
          op[nop++] = Op{g, t / 2, t & 1, blk == 1 ? (t & 1) : blk - 2 + 2 * (t & 1), q, q == 0};
        }
      } else {
        const int u = (g - 59) / 5, k = (g - 59) % 5;
        if (g == 59) s2 = nop;
        for (int t = 0; t < 4; ++t) op[nop++] = Op{g, t, C, 2 * (u & 1), 4 * k + t, k == 0 && t == 0};
        if (k == 4) op[nop++] = Op{-1 - u, 0, 0, 2 * (u & 1), 20, 0};
      }
    }
    o0[NB] = nop;
    int wpos[NG] = {}, xpos[9] = {}, endc[NB] = {}, sb[9] = {}, cnt = 0, fpos = 0, lpos[2] = {};
    if constexpr (C == 1) for (int k = 0; k < 3; ++k) {
      pe[k] = Sched<C - 1>{}.pe[k];
      const int f = pe[k] + FLAG_LAG < NB ? pe[k] + FLAG_LAG : NB - 1;
      pu[pe[k]] = 1 + k; fo[f] = 1 + k;
      if (k < 2) { ck[f + CHK_LAG] = 1 + k; for (int q = 0; q < NLS; ++q) ls[f + CHK_LAG + q] = 1 + NLS * k + q; if (f + CHK_LAG + SUM_LAG < NB - 1) su[f + CHK_LAG + SUM_LAG] = 1 + k; else late = true; }
    }
    for (int b = -1; b < NB; ++b) {
      if (b >= 0) {
        int need = 0;
        for (int g = 0; g <= 2 * b + 2 && g < NG; ++g) if (wpos[g] > need) need = wpos[g];
        for (int a = 0; a < 9; ++a) if (xread(a) <= b && xpos[a] > need) need = xpos[a];
        if (b == BS + 2 && endc[BD + 7] > need) need = endc[BD + 7];
        if (pu[b] && endc[sb[2 * pu[b] - 1]] > need) need = endc[sb[2 * pu[b] - 1]];
        wait[b] = cnt - need;
        cnt += extra(b) + (pu[b] ? 1 : 0);
        wait2[b] = cnt - (su[b] ? lpos[su[b] - 1] : fpos);
        if (fo[b]) fpos = ++cnt;
        if (b > 0 && su[b - 1]) ++cnt;
        if (C == 0) for (int k = 0; k < 3; ++k) if (!pe[k] && sb[2 * k + 1] && need >= endc[sb[2 * k + 1]]) pe[k] = b;
        for (int n = o0[b]; n < o0[b + 1]; ++n) if (n >= s2 + 21 && ((n - s2) % 21 == 5 || (n - s2) % 21 == 9)) { ++cnt; if ((n - s2) % 21 == 9) sb[(n - s2) / 21 - 1] = b; }
      }
      const int row = b < 0 ? NB : b;
      for (int k = 0; k < nit[row]; ++k) { ++cnt; if (it[row][k].kind) xpos[it[row][k].idx] = cnt; else wpos[it[row][k].idx] = cnt; }
      if (b >= 0 && ls[b]) lpos[(ls[b] - 1) / NLS] = cnt += 8 / NLS + ((ls[b] - 1) % NLS == 0);
      if (b >= 0) endc[b] = cnt;
    }
    tailw = cnt + 2 - endc[sb[7]];
  }
};
template <int C> constexpr Sched<C> SCH{};
static_assert(SCH<0>.pe[2] > 0 && SCH<0>.pe[1] + FLAG_LAG + CHK_LAG + NLS <= NB && SUM_LAG != SCH<0>.pe[1] - SCH<0>.pe[0] && SUM_LAG >= NLS && SCH<0>.pe[0] + FLAG_LAG + CHK_LAG + SUM_LAG < NB - 1 && CHK_LAG <= SCH<0>.pe[1] - SCH<0>.pe[0], "schedule");

struct Args { const unsigned short *x, *wo, *w1, *w2; char* ws; unsigned short* out; };

#define IC(v) std::integral_constant<int, (v)>{}
DI void add3(float& x, float& y, float& z, float a, float b, float c, float d, float e, float f) {
  asm("s_nop 0\n\tv_add_f32 %0, %3, %4\n\tv_add_f32 %1, %5, %6\n\tv_add_f32 %2, %7, %8" : "=&v"(x), "=&v"(y), "=&v"(z) : "v"(a), "v"(b), "v"(c), "v"(d), "v"(e), "v"(f));
}
DI void mul6(float* e, float s) {
  asm volatile("s_nop 0\n\tv_mul_f32 %0, %0, %6\n\tv_mul_f32 %1, %1, %6\n\tv_mul_f32 %2, %2, %6\n\tv_mul_f32 %3, %3, %6\n\tv_mul_f32 %4, %4, %6\n\tv_mul_f32 %5, %5, %6"
               : "+v"(e[0]), "+v"(e[1]), "+v"(e[2]), "+v"(e[3]), "+v"(e[4]), "+v"(e[5]) : "v"(s));
}
DI void keep9(float* e) { asm volatile("" : "+v"(e[0]), "+v"(e[1]), "+v"(e[2]), "+v"(e[3]), "+v"(e[4]), "+v"(e[5]), "+v"(e[6]), "+v"(e[7]), "+v"(e[8])); }
DI float fmul(float a, float b) { float d; asm("v_mul_f32 %0, %1, %2" : "=v"(d) : "v"(a), "v"(b)); return d; }
template <bool NEG, int H0, class H> DI void smax(const float (&p)[18], unsigned (&o)[9], H&& hook) {
  constexpr float L2E = NEG ? -1.4426950408889634f : 1.4426950408889634f;
  auto m3 = [](float a, float b, float c) { return NEG ? fminf(fminf(a, b), c) : fmaxf(fmaxf(a, b), c); };
  const float mx = m3(m3(m3(p[0], p[1], p[2]), m3(p[3], p[4], p[5]), m3(p[6], p[7], p[8])), m3(m3(p[9], p[10], p[11]), m3(p[12], p[13], p[14]), p[15]), m3(p[16], p[17], p[17]));
  const float nb = -mx * L2E;
  float e[18];
#pragma unroll
  for (int z = 0; z < 9; ++z) e[z] = __builtin_fmaf(p[z], L2E, nb);
  keep9(e);
  hook(IC(H0));
#pragma unroll
  for (int z = 0; z < 9; ++z) { e[z] = __builtin_amdgcn_exp2f(e[z]); e[9 + z] = __builtin_fmaf(p[9 + z], L2E, nb); }
  keep9(e); keep9(e + 9);
  hook(IC(H0 + 1));
  float s0, s1, s2;
  add3(s0, s1, s2, e[0], e[1], e[2], e[3], e[4], e[5]);
#pragma unroll
  for (int z = 9; z < 18; ++z) e[z] = __builtin_amdgcn_exp2f(e[z]);
  add3(s0, s1, s2, s0, e[6], s1, e[7], s2, e[8]);
  keep9(e + 9);
  hook(IC(H0 + 2));
#pragma unroll
  for (int z = 9; z < 18; z += 3) add3(s0, s1, s2, s0, e[z], s1, e[z + 1], s2, e[z + 2]);
  float is = __builtin_amdgcn_rcpf(s0 + s1 + s2);
  keep(is);
  hook(IC(H0 + 3));
  PIN;
  mul6(e, is); mul6(e + 6, is); mul6(e + 12, is);
  hook(IC(H0 + 4));
#pragma unroll
  for (int z = 0; z < 9; ++z) { o[z] = pk2(e[2 * z], e[2 * z + 1]); keep(o[z]); }
}
template <class H> DI void phi_lin(unsigned pfa, bf16x8 xa0, bf16x8 xa1, bf16x8 xa2, float (&p)[18], H&& hook) {
  bf16x8 w[6];
#pragma unroll
  for (int z = 0; z < 6; ++z) w[z] = ldsfrag(pfa + z * 1024);
  f32x16 pa = mfma(w[0], xa0, zero16()), pb = mfma(w[3], xa0, zero16());
  pa = mfma(w[1], xa1, pa); pb = mfma(w[4], xa1, pb);
  pa = mfma(w[2], xa2, pa); pb = mfma(w[5], xa2, pb);
  keep(pa); keep(pb);
  hook();
#pragma unroll
  for (int z = 0; z < 16; ++z) p[z] = rbf(pa[z]);
  p[16] = rbf(pb[0]); p[17] = rbf(pb[1]);
#pragma unroll
  for (int z = 0; z < 18; ++z) keep(p[z]);
}
DI bf16x8 sel8(unsigned row) {
  const unsigned one = (row & 1) ? 0x3f800000u : 0x00003f80u, q = row >> 1;
  return __builtin_bit_cast(bf16x8, u32x4{q == 0 ? one : 0u, q == 1 ? one : 0u, q == 2 ? one : 0u, q == 3 ? one : 0u});
}

template <int C> DI void body(const Args& A, const int r, const int i, const int j) {
#define S SCH<C>
  char* const ws = A.ws;
  const rsrc_t r1 = __builtin_amdgcn_make_buffer_rsrc((void*)A.w1, 0, D_ * DFF_ * 2, 0x00020000);
  const rsrc_t r2 = __builtin_amdgcn_make_buffer_rsrc((void*)A.w2, 0, D_ * DFF_ * 2, 0x00020000);
  const rsrc_t ro = __builtin_amdgcn_make_buffer_rsrc((void*)A.wo, 0, D_ * D_ * 2, 0x00020000);
  const rsrc_t rw = __builtin_amdgcn_make_buffer_rsrc((void*)ws, 0, (int)WS_P, 0x00020000);
  const rsrc_t rp = __builtin_amdgcn_make_buffer_rsrc((void*)(ws + WS_P), 0, (int)(16 * P_SPLIT), 0x00020000);
  const rsrc_t rxx = __builtin_amdgcn_make_buffer_rsrc((void*)A.x, 0, S_ * D_ * 2, 0x00020000);
  const rsrc_t rout = __builtin_amdgcn_make_buffer_rsrc((void*)A.out, 0, S_ * D_ * 2, 0x00020000);
  const unsigned mr1 = (unsigned)r * 1024u, mr4 = (unsigned)r * 4096u;
  unsigned vx, vq, vw1, vw6 = 0, vw2 = 0, vo = 0;
  {
    const unsigned l = fresh_lane();
    vx = l * 16u + (unsigned)(i * 4 + r) * 36864u;
    vq = l * 16u + (unsigned)j * 147456u + mr1;
    vw1 = ((l >> 3) + 8u * r) * 9216u + swz_ch(l) * 16u + (unsigned)(9 * (j >> 1) + ((j & 1) ? 5 : 0)) * 128u;
  }

  auto issue = [&](auto B_, auto K_) {
    constexpr Item t = S.it[B_.value][K_.value];
    if constexpr (t.kind == 1) dma<HREG + (t.idx & 3) * 16384u + t.hi * 1024u, (unsigned)WS_XN + (4 * t.idx + t.hi) * 1024u>(mr4, vx, rw);
    else {
      constexpr int g = t.idx;
      constexpr unsigned mc = (g % 9) * 8192u + t.hi * 4096u;
      if constexpr (g < 27 && g % 3 < 2) dma<mc, (unsigned)WS_WQ + (2 * (g / 3) + g % 3) * 8192u + t.hi * 4096u>(mr1, vq, rw);
      else if constexpr (g < 27) dma<mc, (64u * (g / 3) + 32u * t.hi) * 9216u>(mr1, vw1, r1);
      else if constexpr (g < 32) dma<mc, (128u * (g - 27) + 64u * t.hi) * 9216u>(mr1, vw6, r1);
      else if constexpr (g < 59) dma<mc, (64u * ((g - 23) % 9) + 32u * t.hi) * 9216u + ((g - 23) / 9) * 128u>(mr1, vw1, r1);
      else if constexpr ((g - 59) % 5 == 4 && t.hi == 1) dma<mc, ((g - 59) / 5) * 128u>(mr1, vo, ro);
      else dma<mc, (64u * ((g - 59) % 5) + 32u * t.hi) * 1152u + ((g - 59) / 5) * 128u>(mr1, vw2, r2);
    }
  };
  sfor<0, S.nit[NB]>([&](auto K_) { issue(std::integral_constant<int, NB>{}, K_); });
  unsigned la[2][2] = {{0, 0}, {0, 0}}, l6[2] = {0, 0}, l2[2] = {0, 0};
  unsigned xb, hw = 0, of = 0, pv = 0;
  {
    const unsigned l = fresh_lane(), q4 = (l & 15) >> 2;
    if (C == 1 && r * 64 + l < 200) *LDSP(u32x4, KVT + (r * 64 + l) * 16) = u32x4{0, 0, 0, 0};
    la[C][0] = opaque((8 * (l >> 5) + q4) * 128 + ((C ^ (q4 >> 1)) * 64) + tr_col(l)); la[C][1] = opaque(la[C][0] + 40960u);
    xb = opaque(HREG + mr4 + l * 16);
  }
  const unsigned hreg = HREG + (unsigned)r * 18432u, hodd = (unsigned)(j & 1) * 2048u;
  auto set_s2 = [&]() {
    const unsigned l = fresh_lane(), q4 = (l & 15) >> 2;
    l2[0] = opaque((4 * (l >> 5) + q4) * 128 + ((C ^ (q4 >> 1)) * 64) + tr_col(l)); l2[1] = opaque(l2[0] + 40960u);
    xb = opaque(hreg + l * 16);
    of = opaque(OFR + (unsigned)r * 3072u + l * 16);
    pv = opaque((unsigned)(((i * 4 + r) * 18 + C) * 32768 + j * 2048) + l * 16);
  };
  auto set_w2 = [&]() {
    const unsigned l = fresh_lane();
    vw2 = opaque(((unsigned)(288 * j + 8 * r) + (l >> 3)) * 1152u + swz_ch(l) * 16u);
    if (C == 1 || WHOD == 0) vo = opaque(((unsigned)(36 * j + 4 * r) + (l >> 5) * 18 + ((l >> 3) & 3)) * 1152u + swz_ch(l) * 16u);
  };

  bf16x8 xn[36], hb[21], f[PD + 1];
  f32x16 acc[4];
  u32x4 wt[3], ep[2];
  bf16x8 xa0, xa1;
  u32x2 xq2;
  auto rdA = [&](auto N_) {
    constexpr Op o = S.op[N_.value];
    if constexpr (o.grp >= 0) {
      constexpr int sl = o.grp % 9, hi = sl >= 5;
      constexpr unsigned off = (sl - 5 * hi) * 8192u;
      if constexpr (o.grp >= 59) f[N_.value % (PD + 1)] = trfrag(l2[hi] + off + o.sub * 2048u, l2[hi] + off + o.sub * 2048u + 1024u);
      else if constexpr (o.grp >= 27 && o.grp < 32) f[N_.value % (PD + 1)] = trfrag(l6[hi] + off + o.sub * 1024u, l6[hi] + off + o.sub * 1024u + 256u);
      else f[N_.value % (PD + 1)] = trfrag(la[o.half][hi] + off + o.sub * 2048u, la[o.half][hi] + off + o.sub * 2048u + 512u);
    }
    if constexpr (N_.value >= S.s2 && N_.value < S.s2 + 21) {
      if constexpr (o.bx < 18) hb[o.bx] = ldsfrag(xb + o.bx * 1024);
      else hb[o.bx] = ldsfrag(of + (o.bx - 18) * 1024);
    }
  };
  constexpr int SEG = C == 0 ? NA : S.s2;
  auto epi_h = [&](auto AC_, auto K_, unsigned adr, u32x4& e) {
    constexpr int k = K_.value;
    e[k & 3] = relu2(pk2(acc[AC_.value][2 * k], acc[AC_.value][2 * k + 1]));
    if constexpr ((k & 3) == 3) *LDSP(u32x4, adr + (k >> 2) * 1024) = e;
  };
  auto epi_p = [&](auto U_, auto K_) {
    constexpr int k = K_.value, u = U_.value;
    ep[0][k & 3] = pk2(acc[2 * (u & 1)][2 * k], acc[2 * (u & 1)][2 * k + 1]);
    if constexpr ((k & 3) == 3) __builtin_amdgcn_raw_buffer_store_b128(ep[0], rp, pv + (k >> 2) * 1024, u * 65536, 16);
  };
  auto vstage = [&]() {
    const unsigned l = fresh_lane(), h = l >> 5, srow = opaque(hreg + 6144u + (l & 31) * 224u);
    *LDSP(u32x2, srow + 144 + C * 40 + h * 8) = u32x2{pk2(acc[1][4], acc[1][5]), pk2(acc[1][6], acc[1][7])};
    *LDSP(u32x2, srow + 144 + C * 40 + 16 + h * 8) = u32x2{pk2(acc[1][8], acc[1][9]), pk2(acc[1][10], acc[1][11])};
    if (h == 0) *LDSP(u32x2, srow + 144 + C * 40 + 32) = u32x2{pk2(acc[1][12], acc[1][13]), 0x00003f80u};
  };
  unsigned fl = 0, fa = 0, pz = 0, sv = 0, re = 0;
  const int cc = r & 1;
  u32x4 rv[2][8];
  u32x2 rx[2];
  auto set_red = [&]() {
    const unsigned l = fresh_lane();
    fa = opaque((l & 15) * 4u);
    pz = opaque(l ? (unsigned)WS_P : 0u);
    sv = opaque((unsigned)(((i * 4 + (j >> 2)) * 18 + cc) * 32768 + (j & 3) * 128) + ((l >> 4) & 1) * 1024 + ((l >> 3) & 1) * 512 + (l & 7) * 16 + (l >> 5) * 2048u);
    re = opaque(((unsigned)(i * 128 + j * 8 + (l & 7)) * D_ + (unsigned)(32 * cc) + ((l >> 4) & 1) * 16 + ((l >> 3) & 1) * 4 + (l >> 5) * 8) * 2u);
  };
  auto pf_load = [&](bool early) { ld_flag(fl, fa, rw, (unsigned)WS_PF + (unsigned)((i * 8 + (early ? 4 : cc * 4) + (j >> 2)) * PF_LINE + (early ? 32 : 0)) * 4u); };
  auto pf_check = [&](bool early, unsigned need) {
    keep(fl);
    for (unsigned spins = 0; __builtin_expect(!__all(fl >= need), 0) && spins < (1u << 22); ++spins) {
      pf_load(early);
      wait_vm<0>();
      keep(fl);
    }
    __builtin_amdgcn_fence(__ATOMIC_ACQUIRE, "wavefront");
  };
  auto pf_pub = [&](bool early, unsigned v) { __builtin_amdgcn_raw_buffer_store_b32(v, rw, pz, (unsigned)WS_PF + (unsigned)((i * 8 + C * 4 + r) * PF_LINE + j + (early ? 32 : 0)) * 4u, 16); };
  auto red_one = [&](int u, int q, int k) {
    if (k < 8) ld_sc1(rv[q][k], sv, rp, (unsigned)k * 4096u + (unsigned)u * 65536u);
    else ld_x(rx[q], re, rxx, (unsigned)u * 128u);
  };
  auto red_load = [&](int u, int q) {
#pragma unroll
    for (int k = 0; k < 9; ++k) red_one(u, q, k);
  };
  bf16x8 rsel;
  auto red_keep = [&](int q) {
#pragma unroll
    for (int k = 0; k < 8; ++k) keep(rv[q][k]);
    keep(rx[q]);
    rsel = sel8(fresh_lane() & 31);
  };
  auto red_step = [&](int k, int q) { acc[1] = mfma(rsel, __builtin_bit_cast(bf16x8, rv[q][k]), k ? acc[1] : zero16()); };
  auto red_out = [&](int u, int q) {
    const unsigned x0 = rx[q][0], x1 = rx[q][1];
    __builtin_amdgcn_raw_buffer_store_b64(u32x2{pk2(acc[1][0] + blo(x0), acc[1][1] + bhi(x0)), pk2(acc[1][2] + blo(x1), acc[1][3] + bhi(x1))}, rout, re, (unsigned)u * 128u, 0);
  };
  auto red_sum = [&](int u, int q) {
    red_keep(q);
#pragma unroll
    for (int k = 0; k < 8; ++k) red_step(k, q);
    red_out(u, q);
  };
  auto side = [&](auto B_, auto N_) {
    constexpr int b = B_.value, n = N_.value, t = n - S.o0[b];
    if constexpr (n < NA - 12 && (n % 12 == 8 || n % 12 == 9)) {
      constexpr int a = n / 12 + 1, k = 2 * (n % 12 - 8);
      xn[4 * a + k] = ldsfrag(xb + (a & 3) * 16384u + k * 1024);
      xn[4 * a + k + 1] = ldsfrag(xb + (a & 3) * 16384u + (k + 1) * 1024);
    }
    if constexpr (b == 9 && t == 6) {
      const unsigned l = fresh_lane();
      vw6 = opaque(((l >> 2) + 16u * r) * 9216u + (l & 3) * 16u + (unsigned)(9 * (j >> 1) + 4) * 128u + ((j & 1) ? 64u : 0u));
    }
    if constexpr (C == 1) {
      if constexpr (b == 6 && t == 0) {
        const unsigned v = (unsigned)(j * 6144) + mr1 + fresh_lane() * 16;
        dma<OFR, (unsigned)WS_PHI>(mr1, v, rw);
        dma<OFR + 4096u, (unsigned)WS_PHI + 4096u>(mr1, v, rw);
      }
      if constexpr (n == NA - 6) { const unsigned l = fresh_lane(); l6[0] = opaque((8 * (l >> 5) + ((l & 15) >> 2)) * 64 + tr_col(l)); }
      if constexpr (n == NA - 4) { xa0 = pack8(acc[0], 0); keep(xa0); }
      if constexpr (n == NA - 3) { xa1 = pack8(acc[0], 1); keep(xa1); }
      if constexpr (n == NA - 2) { xq2[0] = pk2(acc[1][0], acc[1][1]); xq2[1] = pk2(acc[1][2], acc[1][3]); keep(xq2); }
      if constexpr (n == NA - 1) vstage();
      if constexpr (n == NA + 11) hw = opaque(hreg + hodd + fresh_lane() * 16);
      if constexpr (n >= NA + 14 && n < NA + 30 && !((n - NA) & 1)) epi_h(IC(2), IC((n - NA - 14) / 2), hw + 2048u, ep[0]);
      if constexpr (n == NA + 31) { la[0][0] = opaque(la[1][0] ^ 64u); la[0][1] = opaque(la[0][0] + 40960u); }
      if constexpr (n == NA + 69) l6[1] = opaque(hw + 16384u - 9u * hodd);
      if constexpr (n >= NA + 70 && n < NA + 86 && !((n - NA) & 1)) epi_h(IC(3), IC((n - NA - 70) / 2), l6[1], ep[0]);
    } else {
      if constexpr (n >= NA + 74 && n < NA + 90 && !((n - NA) & 1)) epi_h(IC(0), IC((n - NA - 74) / 2), hw + 8192u, ep[0]);
      if constexpr (n >= NA + 90 && n < NA + 106 && !((n - NA) & 1)) epi_h(IC(2), IC((n - NA - 90) / 2), hw + 10240u, ep[0]);
      if constexpr (b == BD + 7 && t == 0) ld3(wt, ((unsigned)(j * 2 + C) * 64u + fresh_lane()) * 48u, rw, (unsigned)WS_WT);
      if constexpr (b == BS - 1 && t == 2) set_s2();
      if constexpr (b == BD + 3 && t == 6) set_w2();
      if constexpr (n == S.s2 + 1) sfor<0, 4>([&](auto K_) { epi_h(IC(1), K_, hw + 12288u, ep[0]); });
      if constexpr (n == S.s2 + 2) { sfor<4, 8>([&](auto K_) { epi_h(IC(1), K_, hw + 12288u, ep[0]); }); sfor<0, 2>([&](auto K_) { epi_h(IC(3), K_, hw + 14336u, ep[1]); }); }
      if constexpr (n == S.s2 + 3) sfor<2, 8>([&](auto K_) { epi_h(IC(3), K_, hw + 14336u, ep[1]); });
    }
    if constexpr (n >= S.s2 + 21) {
      constexpr int u = (n - S.s2) / 21, s = (n - S.s2) % 21;
      if constexpr (s >= 2 && s < 10) epi_p(IC(u - 1), IC(s - 2));
    }
    if constexpr (n == S.s2 + 12) set_red();
    if constexpr (S.ls[b]) {
      constexpr int k = (S.ls[b] - 1) / NLS, q = (S.ls[b] - 1) % NLS;
      if constexpr (t < 8 && t % NLS == NLS / 2) red_one(2 * k + (r >> 1), k, q * (8 / NLS) + t / NLS);
      if constexpr (q == 0 && t == 7) red_one(2 * k + (r >> 1), k, 8);
    }
    if constexpr (S.su[b] && t < 8) red_step(t, S.su[b] - 1);
    if constexpr (b > 0 && S.su[b > 0 ? b - 1 : 0] && t == 1) red_out(2 * (S.su[b > 0 ? b - 1 : 0] - 1) + (r >> 1), S.su[b > 0 ? b - 1 : 0] - 1);
  };
  auto open = [&](auto B_) {
    constexpr int b = B_.value;
    if constexpr (S.wait[b] < 63) wait_vm<S.wait[b]>();
    if constexpr (b == BS + 1) wait_lds();
    if constexpr (b == BS + 2) { keep(wt[0]); keep(wt[1]); keep(wt[2]); }
    bar();
  };
  auto run = [&](auto B_) {
    constexpr int b = B_.value, n0 = S.o0[b], n1 = S.o0[b + 1], ni = S.nit[b], nd = ni ? ni : 1;
    constexpr bool shared = (b > 0 && b <= BB) || b == BD || (b >= BS && SPLACE);
    constexpr auto gap = [](int k) {
      if (shared && PLACE == 1) return C ? -1 : n1 - n0 - 1;
      if (shared && PLACE == 2) return C ? k - 1 : n1 - n0 - ni + k;
      if (shared && PLACE == 3) return C ? -1 : (k * (n1 - n0)) / nd;
      return (k * (n1 - n0)) / nd + (ni * 2 <= n1 - n0 && C ? (n1 - n0) / (2 * nd) : 0);
    };
    open(B_);
    if constexpr (S.pu[b]) pf_pub(true, 2 * S.pu[b]);
    if constexpr (S.ck[b] || S.su[b]) wait_vm<S.wait2[b]>();
    if constexpr (S.su[b]) red_keep(S.su[b] - 1);
    if constexpr (S.ck[b]) { keep(acc[S.op[n0 - 1].acc]); keep(ep[0]); if constexpr (S.su[b - 1]) keep(acc[1]); pf_check(true, 2 * S.ck[b]); }
    if constexpr (S.fo[b]) pf_load(true);
    sfor<0, ni>([&](auto K_) { if constexpr (gap(K_.value) < 0) issue(B_, K_); });
    PIN;
    if constexpr (b == 0) {
      sfor<0, 4>([&](auto T_) { xn[T_.value] = ldsfrag(xb + T_.value * 1024); rdA(T_); });
      PIN;
    }
    if constexpr (n0 == SEG) { sfor<SEG, SEG + PD>(rdA); PIN; }
    sfor<n0, n1>([&](auto N_) {
      constexpr int n = N_.value, t = n - n0;
      constexpr Op o = S.op[n];
      bf16x8 a;
      if constexpr (o.grp >= 0) a = f[n % (PD + 1)];
      else { const unsigned tw = wt[(-1 - o.grp) >> 2][(-1 - o.grp) & 3]; a = __builtin_bit_cast(bf16x8, u32x4{tw, 0, 0, 0}); }
      if constexpr (n >= S.s2) acc[o.acc] = mfma(a, hb[o.bx], o.first ? zero16() : acc[o.acc]);
      else acc[o.acc] = mfma(a, xn[o.bx], o.first ? zero16() : acc[o.acc]);
      PIN;
      side(B_, N_);
      PIN;
      if constexpr (n + PD < S.nop && !(n < SEG && n + PD >= SEG)) rdA(IC(n + PD));
      sfor<0, ni>([&](auto K_) { if constexpr (gap(K_.value) == t) issue(B_, K_); });
      PIN;
    });
  };
  auto one = [&](auto B_, auto K_) { PIN; issue(B_, K_); PIN; };
  auto hook = [&](auto H_) {
    constexpr int b = (C == 0 ? BB : BD) + 1 + H_.value / 4, k = H_.value % 4;
    if constexpr (k == 0) open(IC(b));
    if constexpr (S.nit[b] == 4) one(IC(b), IC(k)); else if constexpr (S.nit[b] == 2 && !(k & 1)) one(IC(b), IC(k / 2)); else PIN;
  };

  sfor<0, BB + 1>(run);
  if constexpr (C == 1) {
    __builtin_amdgcn_s_setprio(PRB == 1);
    sfor<BB + 1, BD + 1>(run);
    __builtin_amdgcn_s_setprio(PRD == 1);
  } else {
    __builtin_amdgcn_s_setprio(PRB == 2);
    float p[18];
    unsigned ph[9];
    const unsigned lane = fresh_lane(), h = lane >> 5, m = lane & 31, srow = opaque(hreg + 6144u + m * 224u + h * 72);
    phi_lin(opaque(OFR + lane * 16), pack8(acc[0], 0), pack8(acc[0], 1), __builtin_bit_cast(bf16x8, u32x4{pk2(acc[1][0], acc[1][1]), pk2(acc[1][2], acc[1][3]), 0, 0}), p,
            [&]() { hook(IC(0)); vstage(); hook(IC(1)); });
    hook(IC(2));
    if constexpr (!W2PRE) {
      const unsigned n = min((unsigned)(i * 184 + r * 46) + min(lane, 45u), 2915u);
      touch<KVT + 3328>(n < 2592 ? (const char*)A.w2 + (size_t)j * 331776 + n * 128 : (const char*)A.wo + (size_t)j * 41472 + (n - 2592) * 128);
    }
    smax<false, 3>(p, ph, hook);
#pragma unroll
    for (int g = 0; g < 4; ++g) *LDSP(u32x2, srow + g * 8) = u32x2{ph[2 * g], ph[2 * g + 1]};
    const unsigned last = ph[8];
    hook(IC(8));
    smax<true, 9>(p, ph, hook);
    *LDSP(u32x2, srow + 32) = u32x2{last, ph[0]};
#pragma unroll
    for (int g = 0; g < 4; ++g) *LDSP(u32x2, srow + 40 + g * 8) = u32x2{ph[2 * g + 1], ph[2 * g + 2]};
    hook(IC(14));
    const int hd = r >> 1, ft = r & 1;
    const unsigned tb = HREG + 6144u + (8 * h + ((lane & 15) >> 2)) * 224 + tr_col(lane), ta = opaque(tb + hd * 72 + ft * 64), tv = opaque(tb + 144 + hd * 40);
    const unsigned ob = opaque(m < 19 ? (unsigned)((int)WS_KV + (j * 16 + i) * (int)KV_BYTES + hd * 1520 + ft * 64) + m * 80 + h * 8 : (unsigned)WS_P);
    hook(IC(15));
    wait_lds();
    hook(IC(16));
    {
      bf16x8 fa[8], fb[8];
      f32x16 kv = zero16();
      auto rd = [&](int s) { const unsigned o = (s >> 1) * 18432u + (s & 1) * 3584u; fa[s] = trfrag(ta + o, ta + o + 896); fb[s] = trfrag(tv + o, tv + o + 896); };
      rd(0); rd(1); rd(2);
      sfor<0, 8>([&](auto T_) {
        constexpr int s = T_.value;
        if constexpr (s == 1 || s == 4) hook(IC(17 + s / 4));
        kv = mfma(fa[s], fb[s], kv);
        if constexpr (s + 3 < 8) rd(s + 3);
        PIN;
      });
      hook(IC(19));
      const bool pad = ft == 1 && h == 1;
      __builtin_amdgcn_raw_buffer_store_b64(u32x2{pad ? 0u : pk2(kv[0], kv[1]), pad ? 0u : pk2(kv[2], kv[3])}, rw, ob, 0, 16);
      if (ft == 0) {
#pragma unroll
        for (int g = 1; g < 4; ++g) __builtin_amdgcn_raw_buffer_store_b64(u32x2{pk2(kv[4 * g], kv[4 * g + 1]), pk2(kv[4 * g + 2], kv[4 * g + 3])}, rw, ob + g * 16, 0, 16);
      }
    }
    hook(IC(20));
    hw = opaque(hreg + hodd + lane * 16);
    la[1][0] = opaque(la[0][0] ^ 64u); la[1][1] = opaque(la[1][0] + 40960u);
    sfor<0, 8>([&](auto K_) { if constexpr (K_.value % 3 == 1) { __builtin_amdgcn_s_sleep(1); hook(IC(21 + K_.value / 3)); } epi_h(IC(2), K_, hw, ep[0]); });
    __builtin_amdgcn_s_setprio(PRD == 2);
    run(IC(BD));
  }
  if constexpr (C == 0) sfor<BD + 1, BS>(run);
  else {
    const int lane = (int)fresh_lane(), h = lane >> 5, m = lane & 31;
    float p[18];
    unsigned pp[9], pn[9];
    hook(IC(0));
    {
      const unsigned q0 = xq2[0], q1 = xq2[1];
      phi_lin(opaque(OFR + lane * 16), xa0, xa1, __builtin_bit_cast(bf16x8, u32x4{q0, q1, 0, 0}), p, [&]() {
        hook(IC(1));
        sfor<0, 8>([&](auto K_) { epi_h(IC(0), K_, hw + 4096u, ep[0]); });
        hook(IC(2));
        hook(IC(3));
      });
    }
    hook(IC(4));
    u32x4 ld[12];
    const unsigned gv = opaque((unsigned)((j * 16 + (lane >> 4)) * (int)KV_BYTES + r * 768 + (lane & 15) * 16));
    auto gather = [&](int t) {
#pragma unroll
      for (int ii = 0; ii < 4; ++ii) ld_sc1(ld[4 * t + ii], gv, rw, (unsigned)(WS_KV + 4 * ii * KV_BYTES + t * 256));
    };
    smax<false, 5>(p, pp, [&](auto H_) { hook(H_); if constexpr (H_.value >= 8) gather(H_.value - 8); });
    hook(IC(10));
    gather(2);
    hook(IC(11));
    smax<true, 12>(p, pn, hook);
    hook(IC(17));
    sfor<0, 8>([&](auto K_) { epi_h(IC(1), K_, hw + 6144u, ep[0]); });
    hook(IC(18));
    const bf16x8 sel = sel8(lane & 15);
    const int hd = (m >> 2) & 1;
    const unsigned ra = opaque(KVT + (unsigned)(hd == h ? h * 19 + (m & 3) + 4 * (m >> 3) : 38) * 80u);
    const unsigned rbb = opaque(KVT + (unsigned)((hd == h && (m >> 3) == 0 && (m & 3) < 3) ? h * 19 + 16 + (m & 3) : 38) * 80u);
    hook(IC(19));
    set_s2();
    hook(IC(20));
    wait_vm<WHOD == 0 ? 0 : WHOD == 1 ? 10 : 5>();
    f32x4 ks[3];
    for (unsigned spins = 0;; ++spins) {
#pragma unroll
      for (int ii = 0; ii < 12; ++ii) keep(ld[ii]);
#pragma unroll
      for (int ii = 0; ii < 4; ++ii)
#pragma unroll
        for (int t = 0; t < 3; ++t)
          ks[t] = __builtin_amdgcn_mfma_f32_16x16x32_bf16(sel, __builtin_bit_cast(bf16x8, ld[4 * t + ii]), ii ? ks[t] : f32x4{0.f, 0.f, 0.f, 0.f}, 0, 0, 0);
      const float chk = (ks[0][0] + ks[1][0] + ks[2][0]) * 0.f;
      if (__builtin_expect(__all(chk == 0.f), 1)) break;
      bool fill = false;
#pragma unroll
      for (int ii = 0; ii < 12; ++ii)
#pragma unroll
        for (int e = 0; e < 4; ++e) { const unsigned a = ld[ii][e]; fill |= a == 0xffffffffu; }
      if (!__any(fill) || spins > (1u << 20)) break;
      gather(0); gather(1); gather(2);
      wait_vm<0>();
    }
    hook(IC(21));
    if (lane < 32) {
      const unsigned kw = opaque(KVT + (unsigned)(r * 768 + (lane & 15) * 16 + (lane >> 4) * 8));
#pragma unroll
      for (int t = 0; t < 3; ++t) *LDSP(u32x2, kw + t * 256) = u32x2{pk2(ks[t][0], ks[t][1]), pk2(ks[t][2], ks[t][3])};
    }
    hook(IC(22));
    __builtin_amdgcn_s_sleep(1);
    hook(IC(23));
    wait_lds();
    hook(IC(24));
    ld3(wt, ((unsigned)(j * 2 + C) * 64u + (unsigned)lane) * 48u, rw, (unsigned)WS_WT);
    f32x16 na = zero16(), nb = zero16();
    {
      const u32x4 pq[5] = {{pp[0], pp[1], pp[2], pp[3]}, {pp[4], pp[5], pp[6], pp[7]}, {pp[8], pn[0], pn[1], pn[2]}, {pn[3], pn[4], pn[5], pn[6]}, {pn[7], pn[8], 0, 0}};
      bf16x8 ka[5], kb[5];
#pragma unroll
      for (int s = 0; s < 5; ++s) { ka[s] = ldsfrag(ra + 16 * s); kb[s] = ldsfrag(rbb + 16 * s); }
      hook(IC(25));
#pragma unroll
      for (int s = 0; s < 5; ++s) {
        na = mfma(ka[s], __builtin_bit_cast(bf16x8, pq[s]), na);
        nb = mfma(kb[s], __builtin_bit_cast(bf16x8, pq[s]), nb);
        if (s == 2) hook(IC(26));
      }
      keep(na); keep(nb);
      hook(IC(27));
    }
    hook(IC(28));
    {
      const float den = rbf(rbf(nb[2]) + 1e-6f), id = __builtin_amdgcn_rcpf(den);
#pragma unroll
      for (int z = 0; z < 8; ++z) na[z] = fmul(rbf(na[z]), id);
      *LDSP(bf16x8, of) = pack8(na, 0);
      hook(IC(29));
#pragma unroll
      for (int z = 8; z < 16; ++z) na[z] = fmul(rbf(na[z]), id);
      *LDSP(bf16x8, of + 1024) = pack8(na, 1);
      hook(IC(30));
      *LDSP(u32x4, of + 2048) = u32x4{pk2(rbf(nb[0]) * id, rbf(nb[1]) * id), 0, 0, 0};
    }
    hook(IC(31));
  }
  __builtin_amdgcn_s_setprio(PRS == C + 1);
  sfor<BS, NB>(run);
  sfor<0, 8>([&](auto K_) { epi_p(IC(8), K_); });
  wait_vm<S.tailw>();
  pf_pub(false, 8);
  if constexpr (C == 1) {
    wait_vm<3>();
    pf_check(true, 6);
    red_load(4 + (r >> 1), 0);
    wait_vm<10>();
    pf_pub(false, 9);
    if constexpr (S.late) red_sum(2 + (r >> 1), 1);
    wait_vm<S.late ? 2 : 1>();
    red_sum(4 + (r >> 1), 0);
  } else {
    wait_vm<1>();
    pf_pub(false, 9);
    pf_load(false);
    wait_vm<0>();
    pf_check(false, 8);
    red_load(6 + (r >> 1), 0);
    if (r < 2) {
      pf_check(false, 9);
      red_load(8, 1);
      wait_vm<9>();
    } else wait_vm<0>();
    red_sum(6 + (r >> 1), 0);
    if (r < 2) {
      wait_vm<1>();
      red_sum(8, 1);
    }
  }
}

#undef S
__global__ __launch_bounds__(512, 2) void k_main(const unsigned short* __restrict__ x, const unsigned short* __restrict__ wo,
                                                 const unsigned short* __restrict__ w1, const unsigned short* __restrict__ w2, char* ws, unsigned short* out) {
  extern __shared__ __attribute__((aligned(16))) char smem[];
  const int w = __builtin_amdgcn_readfirstlane(threadIdx.x >> 6);
  const int xcd = blockIdx.x & 7, idx = blockIdx.x >> 3;
  constexpr int NXJ = 16 / JX;
  const int j = (xcd % NXJ) * JX + (idx % JX), i = (xcd / NXJ) * (32 / JX) + idx / JX;
  const Args A{x, wo, w1, w2, ws, out};
  if (w < 4) body<0>(A, w, i, j); else body<1>(A, w - 4, i, j);
}

__global__ __launch_bounds__(1024) void k_pre(const unsigned short* __restrict__ x, const unsigned short* __restrict__ wqkv,
                                             const unsigned short* __restrict__ wphi, const unsigned short* __restrict__ wo,
                                             const unsigned short* __restrict__ w1, const unsigned short* __restrict__ w2, char* ws, const unsigned short* out) {
  const int tid = threadIdx.x, lane = tid & 63, w = __builtin_amdgcn_readfirstlane(tid >> 6);
  const int xcd = blockIdx.x & 7, idx = blockIdx.x >> 3;
  if (w < 4) {
    const int row = blockIdx.x * 8 + w * 2 + (lane >> 5), li = lane & 31;
    const bool act = li < 24;
    const char* xb = (const char*)x + (size_t)row * 1152 + (act ? li : 0) * 16;
    u32x4 xr[3];
#pragma unroll
    for (int g = 0; g < 3; ++g) xr[g] = *(const u32x4*)(xb + g * 384);
    float v[24], s1 = 0.f;
#pragma unroll
    for (int g = 0; g < 3; ++g)
#pragma unroll
      for (int e = 0; e < 4; ++e) { const unsigned t = xr[g][e]; v[8 * g + 2 * e] = blo(t); v[8 * g + 2 * e + 1] = bhi(t); }
#pragma unroll
    for (int z = 0; z < 24; ++z) s1 += v[z];
    s1 = half_sum(act ? s1 : 0.f);
    const float mean = s1 * (1.f / 576.f);
    float s2 = 0.f;
#pragma unroll
    for (int z = 0; z < 24; ++z) { const float d = v[z] - mean; s2 = __builtin_fmaf(d, d, s2); }
    s2 = half_sum(act ? s2 : 0.f);
    const float sd = rbf(__builtin_sqrtf(rbf(rbf(s2 * (1.f / 576.f)) + 1e-5f)));
    const float rs = 1.f / sd, mb = rbf(mean);
    char* ob = ws + WS_XN + (size_t)(row >> 5) * 36864 + (size_t)(li >> 1) * 1024 + ((li & 1) * 32 + (row & 31)) * 16;
    if (act) {
#pragma unroll
      for (int g = 0; g < 3; ++g) {
        u32x4 o;
#pragma unroll
        for (int e = 0; e < 4; ++e) o[e] = pk2(rbf(v[8 * g + 2 * e] - mb) * rs, rbf(v[8 * g + 2 * e + 1] - mb) * rs);
        *(u32x4*)(ob + g * 12288) = o;
      }
    }
  } else if (w >= 7) {
    const unsigned n = blockIdx.x * 576u + (unsigned)(tid - 448), row = n >> 8, j = (n >> 4) & 15, pt = (n >> 3) & 1, ch = n & 7;
    const unsigned so = pt == 0 ? (ch < 4 ? 1152u + 16u * ch : 16u * (ch - 4))
                                : (ch == 0 ? 1152u + 64u : ch < 4 ? 2304u + 16u * (ch - 1) : ch == 4 ? 64u : 2304u + 36u + 16u * (ch - 5));
    const rsrc_t rq = __builtin_amdgcn_make_buffer_rsrc((void*)wqkv, 0, D_ * NQKV_ * 2, 0x00020000);
    const u32x4 v = __builtin_amdgcn_raw_buffer_load_b128(rq, (int)(row * 3456u + 72u * j + so), 0, 0);
    *(u32x4*)(ws + WS_WQ + j * 147456u + (2 * (row >> 6) + pt) * 8192u + (row & 63) * 128u + (ch ^ (((row >> 1) & 1) << 2)) * 16u) = v;
  } else {
    const int t = tid - 256;
    { const unsigned fv = t < 190 ? ~0u : 0u; *(u32x4*)(ws + WS_KV + blockIdx.x * KV_BYTES + t * 16) = u32x4{fv, fv, fv, fv}; }
    {
      constexpr int NXJ = 16 / JX;
      const int j = (xcd % NXJ) * JX + (idx % JX), i = (xcd / NXJ) * (32 / JX) + idx / JX;
      const int n = i * 180 + min(t, 179), rr = n / 5, l = n - rr * 5;
      touch<0>((const char*)w1 + (size_t)rr * 9216 + (9 * (j >> 1) + ((j & 1) ? 4 : 0) + l) * 128);
      if constexpr (W2PRE) {
        const unsigned q = min(blockIdx.x * 192u + t, 46655u);
        touch<0>(q < 41472u ? (const char*)w2 + q * 128u : (const char*)wo + (q - 41472u) * 128u);
      }
    }
    if (blockIdx.x < 32) {
      const int n = blockIdx.x * 192 + t, j = n / 384, tt = n - j * 384, m = tt & 31, h = (tt >> 5) & 1;
      const int fi = tt >> 6, tile = fi / 3, s = fi - 3 * tile, head = (m >> 2) & 1;
      const int f = tile == 0 ? (m & 3) + 4 * (m >> 3) : 16 + (m & 3);
      const bool rowok = tile == 0 || ((m & 3) < 2 && (m >> 3) == 0);
      s16x8 v;
#pragma unroll
      for (int jj = 0; jj < 8; ++jj) {
        const int sg = 16 * s + 8 * (jj >> 2) + 4 * h + (jj & 3), d = sg - 18 * head;
        const bool ok = rowok && d >= 0 && d < 18;
        const unsigned short tv = wphi[(2 * j + head) * 324 + (ok ? d * 18 + f : 0)];
        v[jj] = ok ? (short)tv : (short)0;
      }
      *(s16x8*)(ws + WS_PHI + (size_t)n * 16) = v;
    } else if (blockIdx.x >= 128) {
      if (t < 48) ((unsigned*)(ws + WS_PF))[(blockIdx.x - 128) * PF_LINE + t] = 0u;
    } else if (blockIdx.x >= 64) {
      if (idx < 10) touch<0>(main_code() + min((unsigned)((idx & 1) * 192 + t), CODE_LINES - 1) * 128u);
      else if (TLBW && idx < 15) {
        const unsigned q = (idx - 10) * 192u + t;
        touch<0>(q < 704u ? ws + q * 65536u : q < 785u ? (const char*)w2 + (q - 704u) * 65536u : q < 866u ? (const char*)w1 + (q - 785u) * 65536u
                 : q < 902u ? (const char*)out + (q - 866u) * 65536u : q < 938u ? (const char*)x + (q - 902u) * 65536u : (const char*)wo + min(q - 938u, 10u) * 65536u);
      }
    } else {
      const int n = (blockIdx.x - 32) * 192 + t, qd = n % 3, ln = (n / 3) & 63, jc = n / 192;
      const unsigned short* src = wo + (size_t)(36 * (jc >> 1) + 18 * (ln >> 5) + 16) * D_ + 32 * (jc & 1) + (ln & 31);
      u32x4 v;
#pragma unroll
      for (int e = 0; e < 4; ++e) { const int u = min(4 * qd + e, 8); v[e] = (unsigned)src[64 * u] | ((unsigned)src[64 * u + D_] << 16); }
      *(u32x4*)(ws + WS_WT + (size_t)n * 16) = v;
    }
    asm volatile("s_waitcnt vmcnt(0)" ::: "memory");
  }
}

extern "C" void kernel_launch(void* const* d_in, const int* in_sizes, int n_in, void* d_out, int out_size, void* d_ws, size_t ws_size,
                              hipStream_t stream) {
  (void)hipFuncSetAttribute((const void*)k_main, hipFuncAttributeMaxDynamicSharedMemorySize, (int)LDS_TOTAL);
  k_pre<<<256, 1024, 256, stream>>>((const unsigned short*)d_in[0], (const unsigned short*)d_in[1], (const unsigned short*)d_in[2],
                                 (const unsigned short*)d_in[3], (const unsigned short*)d_in[5], (const unsigned short*)d_in[6], (char*)d_ws, (const unsigned short*)d_out);
  k_main<<<256, 512, LDS_TOTAL, stream>>>((const unsigned short*)d_in[0], (const unsigned short*)d_in[3],
                                          (const unsigned short*)d_in[5], (const unsigned short*)d_in[6], (char*)d_ws, (unsigned short*)d_out);
}
```
